```python
import torch

def _softmax(x, axis=-1):
    return torch.softmax(x, dim=axis)
def _gelu(x):
    return torch.nn.functional.gelu(x, approximate="tanh")
def _silu(x):
    return torch.nn.functional.silu(x)
def _sigmoid(x):
    return torch.sigmoid(x)
def _layernorm(x, axis=-1, eps=1e-5):
    m = x.mean(dim=axis, keepdim=True)
    v = x.var(dim=axis, keepdim=True, unbiased=False)
    return (x - m) / torch.sqrt(v + eps)
def _scatter_add(tgt, idx, src, axis=0):
    return tgt.clone().index_add_(axis, idx, src)
def _segment_sum(src, seg, n):
    out = torch.zeros((n,) + src.shape[1:], dtype=src.dtype)
    return out.index_add_(0, seg, src)
def _pad(x, pw):
    flat = tuple(v for pair in reversed(pw) for v in pair)
    return torch.nn.functional.pad(x, flat)

kernel_name = 'standalone__gqa_block_cached__near_ridge'

def setup_inputs():

    _w = (lambda a: (lambda t: t.to(torch.bfloat16) if t.is_floating_point() else t)(torch.as_tensor(a)))
    B, Sq, D = 8, 144, 448
    Hq, Hkv, Sk, Dh = 16, 8, 1024, 256
    assert Hq % Hkv == 0, (Hq, Hkv)
    return {
        "x": _w((lambda *s: torch.randn(*s, dtype=torch.bfloat16) * 0.8)(B, Sq, D)),
        "k_cache": _w((lambda *s: torch.randn(*s, dtype=torch.bfloat16) * 0.8)(B, Hkv, Sk, Dh)),
        "v_cache": _w((lambda *s: torch.randn(*s, dtype=torch.bfloat16) * 0.05)(B, Hkv, Sk, Dh)),
        "wq": _w((lambda *s: torch.randn(*s, dtype=torch.bfloat16) * 0.8)(D, Hq * Dh) * (D ** -0.5)),
        "wo": _w((lambda *s: torch.randn(*s, dtype=torch.bfloat16) * 0.05)(Hq * Dh, D) * ((Hq * Dh) ** -0.5)),
    }

def reference(x, k_cache, v_cache, wq, wo):
    B, Sq, D = x.shape
    Hq, Dh = 16, 256
    Hkv = k_cache.shape[1]
    rep = Hq // Hkv
    q = (lambda x, *d: x.permute(*d))((x @ wq).reshape(B, Sq, Hq, Dh), 0, 2, 1, 3)
    k = (lambda x, n, axis: torch.repeat_interleave(x, n, dim=axis))(k_cache, rep, axis=1)
    v = (lambda x, n, axis: torch.repeat_interleave(x, n, dim=axis))(v_cache, rep, axis=1)
    scores = torch.einsum("bhqd,bhkd->bhqk", q, k) * (Dh ** -0.5)
    p = _softmax(scores, axis=-1)
    o = torch.einsum("bhqk,bhkd->bhqd", p, v)
    return (lambda x, *d: x.permute(*d))(o, 0, 2, 1, 3).reshape(B, Sq, Hq * Dh) @ wo
```

```cpp
#include <hip/hip_runtime.h>
#include <hip/hip_bf16.h>
typedef __hip_bfloat16 bf16;
constexpr int B=8,SQ=144,D=448,HQ=16,HKV=8,SK=1024,DH=256,NH=HQ*DH;
__device__ __forceinline__ float b2f(bf16 x){return __bfloat162float(x);}
__global__ void naive_q(const bf16* x,const bf16* wq,float* q){
  int row=blockIdx.x; int c=blockIdx.y*256+threadIdx.x;
  float a=0; for(int k=0;k<D;++k) a+=b2f(x[row*D+k])*b2f(wq[(size_t)k*NH+c]);
  q[(size_t)row*NH+c]=b2f(__float2bfloat16(a));
}
__global__ void naive_attn(const float* q,const bf16* k,const bf16* v,float* o){
  __shared__ float qs[DH]; __shared__ float s[SK]; __shared__ float red[256];
  int row=blockIdx.x, h=blockIdx.y, b=row/SQ, kvh=h/2, t=threadIdx.x;
  qs[t]=q[(size_t)row*NH+h*DH+t]; __syncthreads();
  const bf16* kb=k+((size_t)(b*HKV+kvh))*SK*DH; const bf16* vb=v+((size_t)(b*HKV+kvh))*SK*DH;
  float m=-1e30f;
  for(int j=t;j<SK;j+=256){float a=0; for(int d=0;d<DH;++d) a+=qs[d]*b2f(kb[(size_t)j*DH+d]); a*=0.0625f; s[j]=a; m=fmaxf(m,a);}
  red[t]=m; __syncthreads(); for(int w=128;w>0;w>>=1){if(t<w)red[t]=fmaxf(red[t],red[t+w]); __syncthreads();} m=red[0]; __syncthreads();
  float l=0; for(int j=t;j<SK;j+=256){float e=__expf(s[j]-m); s[j]=e; l+=e;}
  red[t]=l; __syncthreads(); for(int w=128;w>0;w>>=1){if(t<w)red[t]+=red[t+w]; __syncthreads();} l=red[0];
  float a=0; for(int j=0;j<SK;++j) a+=s[j]*b2f(vb[(size_t)j*DH+t]);
  o[(size_t)row*NH+h*DH+t]=b2f(__float2bfloat16(a/l));
}
__global__ void naive_o(const float* o,const bf16* wo,bf16* out){
  int row=blockIdx.x; int c=threadIdx.x; if(c>=D) return;
  float a=0; for(int k=0;k<NH;++k) a+=o[(size_t)row*NH+k]*b2f(wo[(size_t)k*D+c]);
  out[row*D+c]=__float2bfloat16(a);
}
extern "C" void kernel_launch(void* const* d_in, const int* in_sizes, int n_in, void* d_out, int out_size, void* d_ws, size_t ws_size, hipStream_t stream){
  const bf16* x=(const bf16*)d_in[0]; const bf16* k=(const bf16*)d_in[1]; const bf16* v=(const bf16*)d_in[2];
  const bf16* wq=(const bf16*)d_in[3]; const bf16* wo=(const bf16*)d_in[4];
  float* q=(float*)d_ws; float* o=q+(size_t)B*SQ*NH;
  naive_q<<<dim3(B*SQ,NH/256),256,0,stream>>>(x,wq,q);
  naive_attn<<<dim3(B*SQ,HQ),256,0,stream>>>(q,k,v,o);
  naive_o<<<B*SQ,448,0,stream>>>(o,wo,(bf16*)d_out);
}
```

```cpp
#include <hip/hip_runtime.h>
#include <hip/hip_bf16.h>
#include <type_traits>

typedef short bf16x8 __attribute__((ext_vector_type(8)));
typedef short s16x4 __attribute__((ext_vector_type(4)));
typedef float f32x4 __attribute__((ext_vector_type(4)));
typedef float f32x2 __attribute__((ext_vector_type(2)));
typedef __bf16 bf16x2_t __attribute__((ext_vector_type(2)));
typedef unsigned u32x2 __attribute__((ext_vector_type(2)));
typedef unsigned u32x4 __attribute__((ext_vector_type(4)));
typedef unsigned long long u64;
#define AS3 __attribute__((address_space(3)))
#define DI __device__ __forceinline__

constexpr int SQ = 144, HKV = 8, SK = 1024;
constexpr float QSCALE = 0.0625f * 1.4426950408889634f;
constexpr float THR = 8.0f;

constexpr unsigned KB = 1024;
constexpr unsigned L_KR = 0;
constexpr unsigned L_XB = 16 * KB;
constexpr unsigned L_PA = 64 * KB;
constexpr unsigned L_PB = 144 * KB;
constexpr unsigned L_O4 = 150 * KB;
constexpr unsigned L_Q4 = 64 * KB;
constexpr unsigned L_MISC = 79 * KB;
constexpr unsigned L_VR = 80 * KB;
constexpr unsigned L_WS = 80 * KB;
constexpr unsigned L_QX = 112 * KB;
constexpr unsigned LDS_BYTES = 160 * KB;
constexpr unsigned CODE_KB = 38;

DI unsigned lane_id() { unsigned l; asm volatile("v_mbcnt_lo_u32_b32 %0, -1, 0\n\tv_mbcnt_hi_u32_b32 %0, -1, %0" : "=v"(l)); return l; }
DI void bcast_set(f32x4& t, float aone, float v) { asm volatile("s_nop 1\n\tv_mfma_f32_16x16x4_f32 %0, %1, %2, 0" : "+v"(t) : "v"(aone), "v"(v)); }
DI void bcast_add(f32x4& t, float aone, float v) { asm volatile("s_nop 1\n\tv_mfma_f32_16x16x4_f32 %0, %1, %2, %0" : "+v"(t) : "v"(aone), "v"(v)); }
DI void hold5(f32x4& a, f32x4& b, f32x4& c, f32x4& d, f32x4& e) { asm volatile("s_nop 7\n\ts_nop 3" : "+v"(a), "+v"(b), "+v"(c), "+v"(d), "+v"(e)); }
DI float mulv(float a, float b) { float r; asm volatile("v_mul_f32_e32 %0, %1, %2" : "=v"(r) : "v"(a), "v"(b)); return r; }
template <int V> using IC = std::integral_constant<int, V>;
template <int I, int N, class F> DI void static_for(F&& f) {
  if constexpr (I < N) { f(IC<I>{}); static_for<I + 1, N>(f); }
}
template <int N> DI void waitv() { asm volatile("s_waitcnt vmcnt(%0)" ::"i"(N) : "memory"); }
DI void bar() { __builtin_amdgcn_sched_barrier(0); asm volatile("" ::: "memory"); __builtin_amdgcn_s_waitcnt(0xC07F); asm volatile("s_barrier" ::: "memory"); __builtin_amdgcn_sched_barrier(0); }
#define SBAR() __builtin_amdgcn_sched_barrier(0)
#define PIN(x) asm volatile("" : "+v"(x))
DI void dma16(unsigned voff, const void* sbase, unsigned ldsaddr) {
  asm volatile("s_mov_b32 m0, %2\n\ts_nop 0\n\tglobal_load_lds_dwordx4 %0, %1" ::"v"(voff), "s"(sbase), "s"(ldsaddr) : "memory");
}
DI void dma4(unsigned voff, const void* sbase, unsigned ldsaddr) {
  asm volatile("s_mov_b32 m0, %2\n\ts_nop 0\n\tglobal_load_lds_dword %0, %1" ::"v"(voff), "s"(sbase), "s"(ldsaddr) : "memory");
}
DI bf16x8 lds128(unsigned a) { return *(AS3 const bf16x8*)a; }
DI s16x4 ldstr(unsigned a) { return __builtin_amdgcn_ds_read_tr16_b64_v4i16((AS3 s16x4*)a); }
DI bf16x8 cat(s16x4 l, s16x4 h) { return (bf16x8){l[0], l[1], l[2], l[3], h[0], h[1], h[2], h[3]}; }
DI unsigned cvtpk(float lo, float hi) { f32x2 v = {lo, hi}; return __builtin_bit_cast(unsigned, __builtin_convertvector(v, bf16x2_t)); }
DI void ldsw64(unsigned a, unsigned lo, unsigned hi) { *(AS3 u32x2*)a = (u32x2){lo, hi}; }
#define MFMA(a, b, c) __builtin_amdgcn_mfma_f32_16x16x32_bf16((a), (b), (c), 0, 0, 0)
DI unsigned rfl(unsigned v) { return __builtin_amdgcn_readfirstlane(v); }
DI float fmax3(float a, float b, float c) { float r; asm volatile("v_max3_f32 %0, %1, %2, %3" : "=v"(r) : "v"(a), "v"(b), "v"(c)); return r; }
DI float fmax2(float a, float b) { float r; asm volatile("v_max_f32_e32 %0, %1, %2" : "=v"(r) : "v"(a), "v"(b)); return r; }
DI void mfma_acc(f32x4& c, bf16x8 a, bf16x8 b) { asm volatile("v_mfma_f32_16x16x32_bf16 %0, %1, %2, %0" : "+v"(c) : "v"(a), "v"(b)); }
DI void mfma_init(f32x4& c, bf16x8 a, bf16x8 b, f32x4 c0) { asm volatile("v_mfma_f32_16x16x32_bf16 %0, %1, %2, %3" : "=&v"(c) : "v"(a), "v"(b), "v"(c0)); }
template <int IMML, int OFF> DI void mfma_init_dma(f32x4& c, bf16x8 a, bf16x8 b, f32x4 c0, unsigned voff, const void* sbase, unsigned sl) {
  asm volatile("s_add_u32 m0, %5, %6\n\tv_mfma_f32_16x16x32_bf16 %0, %1, %2, %8\n\tglobal_load_lds_dwordx4 %3, %4 offset:%7"
               : "=&v"(c) : "v"(a), "v"(b), "v"(voff), "s"(sbase), "s"(sl), "i"(IMML - OFF), "i"(OFF), "v"(c0) : "memory", "scc");
}
DI void mfma_zero(f32x4& c, bf16x8 a, bf16x8 b) { asm volatile("v_mfma_f32_16x16x32_bf16 %0, %1, %2, 0" : "=&v"(c) : "v"(a), "v"(b)); }
template <int IMML, int OFF> DI void mfma_acc_dma(f32x4& c, bf16x8 a, bf16x8 b, unsigned voff, const void* sbase, unsigned sl) {
  asm volatile("s_add_u32 m0, %5, %6\n\tv_mfma_f32_16x16x32_bf16 %0, %1, %2, %0\n\tglobal_load_lds_dwordx4 %3, %4 offset:%7"
               : "+v"(c) : "v"(a), "v"(b), "v"(voff), "s"(sbase), "s"(sl), "i"(IMML - OFF), "i"(OFF) : "memory", "scc");
}
template <int IMML, int OFF> DI void mfma_zero_dma(f32x4& c, bf16x8 a, bf16x8 b, unsigned voff, const void* sbase, unsigned sl) {
  asm volatile("s_add_u32 m0, %5, %6\n\tv_mfma_f32_16x16x32_bf16 %0, %1, %2, 0\n\tglobal_load_lds_dwordx4 %3, %4 offset:%7"
               : "=&v"(c) : "v"(a), "v"(b), "v"(voff), "s"(sbase), "s"(sl), "i"(IMML - OFF), "i"(OFF) : "memory", "scc");
}


struct QSched { int n; int op[8]; };
constexpr QSched QS[15] = {
  {5, {199, 100, 0, 1, 101}},
  {2, {2, 3}}, {3, {102, 4, 5}}, {3, {103, 6, 104}}, {2, {7, 105}}, {3, {8, 9, 106}}, {0, {}}, {2, {10, 11}}, {0, {}}, {2, {12, 13}}, {1, {200}},
  {2, {201, 202}}, {1, {300}}, {2, {203, 301}}, {0, {}}};
constexpr bool QBAR[15] = {1, 1, 1, 1, 1, 0, 1, 0, 1, 0, 1, 0, 1, 0, 1};
constexpr bool QPRE[15] = {0, 0, 0, 0, 1, 1, 1, 1, 1, 1, 1, 1, 1, 1, 0};
constexpr int qcnt(int op) { return op >= 100 && op < 200 ? 1 : 2; }
constexpr int qwait(int s) {
  int nx = s + 1; while (!QBAR[nx]) ++nx;
  if (!QPRE[nx]) --nx;
  int idx = 0, last = -1;
  for (int r = 0; r <= s; ++r)
    for (int k = 0; k < QS[r].n; ++k) {
      const int op = QS[r].op[k];
      idx += qcnt(op);
      if ((op < 100 && op <= nx) || (op >= 100 && op < 199 && op - 100 <= (nx >> 1)) || op == 199) last = idx - 1;
    }
  return idx - 1 - last;
}
constexpr int qslot(int s, int d) {
  int idx = 0;
  for (int k = 0; k < QS[s + 1].n; ++k) {
    const int op = QS[s + 1].op[k];
    if (d < idx + qcnt(op)) return op * 2 + (d - idx);
    idx += qcnt(op);
  }
  return -1;
}
constexpr int slot_rt(int sl) { return sl < 20 ? ((sl >> 2) + 4) % 5 : sl < 28 ? ((sl & 1) ? 0 : 4) : sl < 36 ? 1 + (sl & 1) : 3; }
constexpr int slot_k(int sl) { return sl < 4 ? (sl ^ 2) : sl < 20 ? (sl & 3) : sl < 36 ? 4 + (((sl - 20) >> 1) & 3) : sl - 32; }
DI float fmax5(float m, f32x4 v) { float r; asm volatile("v_max3_f32 %0, %1, %2, %3\n\tv_max3_f32 %0, %0, %4, %5" : "=&v"(r) : "v"(m), "v"(v[0]), "v"(v[1]), "v"(v[2]), "v"(v[3])); return r; }
DI float fmax4(f32x4 v) { float r; asm volatile("v_max3_f32 %0, %1, %2, %3\n\tv_max_f32_e32 %0, %0, %4" : "=&v"(r) : "v"(v[0]), "v"(v[1]), "v"(v[2]), "v"(v[3])); return r; }
constexpr unsigned wblk(int c, int ks) {
  return c == 0 ? L_KR + (ks & 3) * 16384 + (ks >> 2) * 4096 : c == 1 ? L_KR + (ks & 3) * 16384 + (2 + (ks >> 2)) * 4096
       : c == 2 || c == 6 ? L_VR + ks * 4096
       : c == 3 ? (ks < 3 ? 64 * KB + ks * 4096 : ks == 3 ? 144 * KB : L_KR + (4 + ks) * 4096)
       : c == 4 ? L_KR + ks * 4096 : L_VR + 32768 + ks * 4096;
}

__global__ __launch_bounds__(512, 2) void fused_attn(const char* __restrict__ x, const char* __restrict__ kc, const char* __restrict__ vc,
                                                     const char* __restrict__ wq, const char* __restrict__ wo, char* part, char* __restrict__ out, u64* sync
) {
  extern __shared__ __attribute__((aligned(1024))) char smem[];
  const unsigned L0 = (unsigned)(uintptr_t)smem;
  const int tid = threadIdx.x, lane = tid & 63;
  const int wid = rfl(tid >> 6), role = wid >> 2, w = wid & 3;
  const int bid = blockIdx.x, kvh = bid & 7, slot = bid >> 3, b = slot >> 2, sub = slot & 3, hg = sub >> 1, half = sub & 1, h = 2 * kvh + hg;
  const int r16 = lane & 15, g = lane >> 4, q = (lane >> 2) & 3, p = lane & 3;
  const int rot = (2 * b + half + 3 * hg) % 7;
  const int flip = (2 * b + half) >= 7 && (2 * b + half) < 14;
  auto cbk = [&](int j) { int c = j + rot; return c >= 7 ? c - 7 : c; };
  const int grp = 2 * b + half, roto = grp % 7;
  auto cbo = [&](int j) { int c = j + roto; return c >= 7 ? c - 7 : c; };

  const unsigned vx0 = (lane >> 3) * 896 + (((lane & 7) ^ (lane >> 4)) << 4);
  const char* xrow = x + (size_t)(b * SQ + half * 72) * 896;
  {
    const int j9 = wid > 6 ? 6 : wid;
    dma16(vx0, xrow + 64 * 896 + cbk(j9) * 128, L0 + L_XB + j9 * 9216 + 8192);
  }
  const unsigned vxw = (vx0 ^ ((wid & 1) << 6)) + wid * (8 * 896);
  const unsigned xl = L0 + L_XB + wid * 1024, wl = L0 + L_WS + wid * 2048;
  unsigned vw[4];
#pragma unroll
  for (int v = 0; v < 2; ++v) {
    const int f = (2 * v + (lane >> 5)) | (((wid >> 1) & 1) << 2);
    vw[v] = (4 * wid + 2 * v + (lane >> 5)) * 8192 + (((lane & 31) ^ (f << 1)) << 4);
    vw[2 + v] = vw[v] + (flip ^ 1) * (32 * 8192);
    vw[v] += flip * (32 * 8192);
  }
  const char* wq0 = wq + h * 512;
  auto xsrc = [&](int j) { return xrow + cbk(j) * 128; };
  auto wsrc = [&](int j) { return wq0 + (size_t)cbk(j) * (64 * 8192); };
  dma16(vxw, xsrc(0), xl);
  dma16(vw[0], wsrc(0), wl + 16384); dma16(vw[1], wsrc(0), wl + 16384 + 1024);
  {
    u64 pc; asm volatile("s_getpc_b64 %0" : "=s"(pc));
    const unsigned seg = (unsigned)slot % ((CODE_KB * 8 + 63) / 64);
    if (wid == 7) { unsigned ln = seg * 64 + lane; ln = ln < CODE_KB * 8 ? ln : CODE_KB * 8 - 1; dma4(ln * 128, (const char*)(pc & ~127ull), L0 + 15 * 1024); }
  }
  auto TT = [&](int t) { return t; };
  const char* Kb = kc + (size_t)(b * HKV + kvh) * SK * 512;
  const char* Vb = vc + (size_t)(b * HKV + kvh) * SK * 512;
  const int sr = sub;
  const char* kg = Kb + (size_t)(16 * ((w + sr) & 3)) * 512;
  auto kcb = [&](int j) { return ((j + sr) & 3) * 128; };
  unsigned vk[2];
#pragma unroll
  for (int v = 0; v < 2; ++v) vk[v] = (8 * v + (lane >> 3)) * 512 + (((lane & 7) ^ (4 * v + (lane >> 4))) << 4);
  const unsigned kr = L0 + L_KR + w * 4096, vr = L0 + L_VR + w * 4096;
  const char* vg = Vb + w * 128;
  auto vvoff = [&](int n) {
    const int f = ((lane >> 4) & 1) | ((n & 1) << 1);
    return (unsigned)(((((n >> 1) + sr) & 3) * 16 + 8 * (n & 1) + (lane >> 3)) * 512 + (((lane & 7) ^ (f << 1)) << 4));
  };
  auto wosrc = [&](int c, int ks) { return wo + (size_t)(h * 256 + 32 * ks) * 896 + cbo(c) * 128; };
  auto wovoff = [&](unsigned ln, int pc) {
    const unsigned f = ((ln >> 4) & 1) | ((pc & 1) << 1);
    return (unsigned)((8 * pc + (ln >> 3)) * 896 + (((ln & 7) ^ (f << 1)) << 4));
  };

  const unsigned fa0 = r16 * 128 + ((g ^ (r16 >> 1)) << 4);
  {
    const unsigned fb0 = (r16 & 7) * 128 + ((g ^ ((r16 & 7) >> 1)) << 4);
    const unsigned wa0 = (8 * g + q) * 512 + ((wid * 64 + p * 8) ^ ((q | ((g & 1) << 2)) << 5));
    unsigned wb[2][2], xa[2], xb[2];
#pragma unroll
    for (int u = 0; u < 2; ++u) {
      wb[u][0] = L0 + L_WS + (wa0 ^ (u << 5)); wb[u][1] = wb[u][0] + 49152; xa[u] = L0 + L_XB + (fa0 ^ ((u ^ flip) << 6)); xb[u] = L0 + L_XB + 8192 + (fb0 ^ ((u ^ flip) << 6));
      PIN(wb[u][0]); PIN(wb[u][1]); PIN(xa[u]); PIN(xb[u]);
    }
    const unsigned krq = kr + role * 2048, vrq = vr + role * 2048;
    unsigned vq[4];
#pragma unroll
    for (int n = 0; n < 4; ++n) vq[n] = vvoff(4 * (n >> 1) + 2 * role + (n & 1));
    if (tid < 244) *(AS3 float*)(L0 + L_MISC + tid * 4) = tid < 160 ? -__builtin_inff() : 0.f;
    f32x4 acc[2][5];
    s16x4 al[2][2], ah[2][2]; bf16x8 xf[2][5];
    auto rdq = [&](auto S, auto I) {
      constexpr int s = S.value, i = I.value, sl = (s + 1) % 5;
      if constexpr (i < 4) {
        constexpr int u = i >> 1;
        const unsigned a = wb[u][sl >= 3] + (sl >= 3 ? sl - 3 : sl) * 16384 + (i & 1) * 2048;
        if constexpr (i & 1) ah[s & 1][u] = ldstr(a); else al[s & 1][u] = ldstr(a);
      } else {
        constexpr int rt = i - 4;
        xf[s & 1][rt] = rt < 4 ? lds128(xa[s & 1] + (s >> 1) * 9216 + rt * 2048) : lds128(xb[s & 1] + (s >> 1) * 9216);
      }
    };
    waitv<0>();
    dma16(vw[2], wsrc(0), wl + 32768); dma16(vw[3], wsrc(0), wl + 32768 + 1024);
    dma16(vxw, xsrc(1), xl + 9216);
    static_for<0, 14>([&](auto S) {
      constexpr int s = S.value;
      if constexpr (QBAR[s]) {
        waitv<qwait(s)>();
        bar();
        if constexpr (!QPRE[s]) static_for<0, 9>([&](auto I) { rdq(S, I); });
      }
      static_for<0, 10>([&](auto M) {
        constexpr int m = M.value, u = m & 1, rt = m >> 1;
        const bf16x8 af = cat(al[s & 1][u], ah[s & 1][u]);
        constexpr int ds = (m & 1) == 0 ? qslot(s, m >> 1) : -1;
        constexpr int op = ds >> 1, v = ds & 1;
        auto go = [&](auto IMML, auto OFF, unsigned vo, const char* sb, unsigned sl) {
          if constexpr (s == 0) mfma_zero_dma<IMML.value, OFF.value>(acc[u][rt], af, xf[s & 1][rt], vo, sb, sl);
          else mfma_acc_dma<IMML.value, OFF.value>(acc[u][rt], af, xf[s & 1][rt], vo, sb, sl);
        };
        if constexpr (ds < 0) { if constexpr (s == 0) mfma_zero(acc[u][rt], af, xf[s & 1][rt]); else mfma_acc(acc[u][rt], af, xf[s & 1][rt]); }
        else if constexpr (op < 100) go(IC<((op + 1) % 5) * 16384 + v * 1024>{}, IC<0>{}, vw[2 * (op & 1) + v], wsrc(op >> 1), wl);
        else if constexpr (op < 200) go(IC<(op - 100) * 9216>{}, IC<0>{}, vxw, xsrc(op - 100), xl);
        else if constexpr (op < 300) go(IC<(op - 200) * 16384 + v * 1024>{}, IC<0>{}, vk[v], kg + (size_t)TT((op - 200) >> 1) * 32768 + kcb(2 * ((op - 200) & 1) + role), krq);
        else go(IC<(op - 300) * 16384 + v * 1024>{}, IC<0>{}, vq[2 * (op - 300) + v], vg + (size_t)TT(0) * 32768, vrq);
        if constexpr (QPRE[s + 1] && m < 9) rdq(IC<(s < 13 ? s + 1 : 0)>{}, M);
        SBAR();
      });
    });
    SBAR(); asm volatile("s_nop 7"); SBAR();
    const int kpos = (wid - 2 * sr) & 7;
    unsigned qa = L0 + L_QX + kpos * 1024 + ((g >> 1) * 16 + r16) * 16 + (g & 1) * 8, qb = L0 + L_Q4 + kpos * 512 + ((g >> 1) * 8 + (r16 & 7)) * 16 + (g & 1) * 8;
    PIN(qa); PIN(qb);
#pragma unroll
    for (int u = 0; u < 2; ++u)
#pragma unroll
      for (int rt = 0; rt < 5; ++rt)
        ldsw64(rt < 4 ? qa + rt * 8192 + u * 512 : qb + u * 256,
               cvtpk(acc[u][rt][0] * QSCALE, acc[u][rt][1] * QSCALE), cvtpk(acc[u][rt][2] * QSCALE, acc[u][rt][3] * QSCALE));
  }
  waitv<6>();
  bar();

  u64* tokp = sync + grp * 16;
  u64* inbox = sync + 512 + (size_t)(grp * 16 + h) * 32;
  u64 mytok = 0, tv = 0;
  auto ld64 = [](u64* p) { return __hip_atomic_load(p, __ATOMIC_RELAXED, __HIP_MEMORY_SCOPE_AGENT); };
  auto st64 = [](u64* p, u64 v) { __hip_atomic_store(p, v, __ATOMIC_RELAXED, __HIP_MEMORY_SCOPE_AGENT); };
  auto tokInit = [&]() {
    const unsigned ln = lane_id();
    const u64 v = ld64(ln < 32 ? inbox + ln : tokp + h);
    mytok = __ballot(((v >> (ln < 32 ? ln : 32)) & 1) == 0);
    if (ln == 0) { st64(tokp + h, mytok); *(AS3 u64*)(L0 + L_MISC + 976) = mytok; }
  };
  auto tokLoad = [&]() { tv = ld64(tokp + (lane_id() & 15)); };
  auto echo = [&](int ph0, int ph1) {
    const unsigned ln = lane_id();
    if (ln < 32) st64(sync + 512 + (size_t)(grp * 16 + (ln & 15)) * 32 + (ln < 16 ? ph0 : ph1) * 16 + h, tv);
  };
  auto poll = [&](int ph, int sent) {
    const u64 t0 = __builtin_amdgcn_s_memrealtime();
    u64* pp = inbox + ph * 16 + (lane_id() & 15);
    for (unsigned it = 1;; ++it) {
      const u64 v = ld64(pp);
      if (__all(v == mytok)) break;
      if ((it & 15) == 0) {
        if (__builtin_amdgcn_s_memrealtime() - t0 > 20000000ull) break;
        tokLoad(); echo(0, sent);
      }
      __builtin_amdgcn_s_sleep(1);
    }
  };
  struct OP { unsigned wa, wa1, wa2, oa, ob, la, vo[4], vh[2], so, so4; };
  auto opSetup = [&]() {
    OP o;
    const unsigned ln = lane_id(), r = ln & 15, gg = ln >> 4, qq = (ln >> 2) & 3;
    o.wa = L0 + (((8 * gg + qq) * 128 + ((((qq >> 1) & 1) | ((gg & 1) << 1)) << 5) + (ln & 3) * 8) ^ (w << 5));
    o.wa1 = o.wa + 64 * KB; o.wa2 = o.wa + 128 * KB;
    o.oa = L0 + L_VR + 32768 + ln * 16; o.ob = L0 + L_O4 + (gg * 8 + (r & 7)) * 16;
    o.la = L0 + L_MISC + 640 + r * 4;
    o.so = ln * 8; o.so4 = (gg * 8 + (r & 7)) * 8; PIN(o.so4);
#pragma unroll
    for (int pc = 0; pc < 4; ++pc) { o.vo[pc] = wovoff(ln, pc); PIN(o.vo[pc]); }
    o.vh[0] = o.vo[0] + (wid & 1) * (16 * 896); o.vh[1] = o.vo[1] + (wid & 1) * (16 * 896);
    PIN(o.vh[0]); PIN(o.vh[1]);
    PIN(o.wa); PIN(o.wa1); PIN(o.wa2); PIN(o.oa); PIN(o.ob); PIN(o.la); PIN(o.so);
    return o;
  };
  auto outproj = [&](const OP& op, auto NRT, auto RT0) {
    constexpr int nrt = NRT.value, rt0 = RT0.value;
    bf16x8 of[nrt][8], wf[2][8];
    float li[nrt];
    auto rdw = [&](auto C, auto KS) {
      constexpr unsigned o = wblk(C.value, KS.value);
      const unsigned a = (o < 63 * KB ? op.wa + o : o < 127 * KB ? op.wa1 + (o - 64 * KB) : op.wa2 + (o - 128 * KB));
      return cat(ldstr(a), ldstr(a + 512));
    };
    auto rd0 = [&](auto KS) {
      constexpr int ks = KS.value;
      wf[0][ks] = rdw(IC<0>{}, KS);
#pragma unroll
      for (int r = 0; r < nrt; ++r) {
        const int rt = rt0 + r;
        of[r][ks] = rt < 4 ? lds128(op.oa + (rt >> 1) * 16384 + (ks >> 1) * 4096 + ((rt & 1) * 2 + (ks & 1)) * 1024) : lds128(op.ob + ks * 512);
      }
      SBAR();
    };
    static_for<0, 3>(rd0);
#pragma unroll
    for (int r = 0; r < nrt; ++r) li[r] = *(AS3 const float*)((rt0 + r < 4 ? op.la : op.la & ~32u) + (rt0 + r) * 64);
    char* pbase = part + ((size_t)(h * 16 + grp) * 28 + w) * 2304 + rt0 * 512;
    f32x4 acc[2][nrt]; u32x2 res[2][nrt];
    auto store = [&](int c, const u32x2* rv) {
      char* sb = pbase + cbo(c) * (4 * 2304);
#pragma unroll
      for (int r = 0; r < nrt; ++r)
        __hip_atomic_store((u64*)(sb + (rt0 + r < 4 ? op.so : op.so4) + r * 512), __builtin_bit_cast(u64, rv[r]), __ATOMIC_RELAXED, __HIP_MEMORY_SCOPE_AGENT);
    };
    auto fin = [&](auto C, int r) {
      constexpr int c = C.value;
      const f32x4 a = acc[c & 1][r];
      const float t0 = mulv(a[0], li[r]), t1 = mulv(a[1], li[r]), t2 = mulv(a[2], li[r]), t3 = mulv(a[3], li[r]);
      res[c == 0 ? 0 : 1][r] = (u32x2){cvtpk(t0, t1), cvtpk(t2, t3)};
      PIN(res[c == 0 ? 0 : 1][r]);
    };
    const int hb = wid >> 1;
    const unsigned l3a = L0 + (hb < 3 ? 64 * KB + hb * 4096 : 144 * KB) + (wid & 1) * 2048, l3b = L0 + L_KR + (8 + hb) * 4096 + (wid & 1) * 2048;
    const unsigned lw = L0 + wid * 4096;
    static_for<0, 7>([&](auto C) {
      constexpr int c = C.value;
      if constexpr (c >= 1) {
        if constexpr (c >= 2) waitv<(c == 2 ? 8 : c == 3 ? 8 + 2 * nrt : c == 4 ? 4 + 3 * nrt : c == 5 ? 4 * nrt : nrt)>();
        bar();
        if constexpr (c == 6 && rt0 == 0) { if (wid == 3) echo(0, 0); }
      }
      static_for<0, 8>([&](auto KS) {
        constexpr int ks = KS.value;
        static_for<0, nrt>([&](auto R) {
          constexpr int r = R.value;
          constexpr int di = r == 1 ? (c == 1 && ks >= 2 ? ks + 2 : ks) : (r == 0 && c == 1 && (ks == 2 || ks == 3)) ? ks : -1;
          constexpr int nd = c == 0 ? 2 : c == 1 ? 10 : c == 2 ? 4 : 0;
          auto go = [&](auto IMML, unsigned vo, const char* sb, unsigned sl) {
            if constexpr (ks == 0) mfma_zero_dma<IMML.value, 0>(acc[c & 1][r], wf[c & 1][ks], of[r][ks], vo, sb, sl);
            else mfma_acc_dma<IMML.value, 0>(acc[c & 1][r], wf[c & 1][ks], of[r][ks], vo, sb, sl);
          };
          if constexpr (di < 0 || di >= nd) { if constexpr (ks == 0) mfma_zero(acc[c & 1][r], wf[c & 1][ks], of[r][ks]); else mfma_acc(acc[c & 1][r], wf[c & 1][ks], of[r][ks]); }
          else if constexpr (c == 0) go(IC<di * 1024>{}, op.vh[di], wosrc(3, hb), l3a);
          else if constexpr (c == 1 && di < 2) go(IC<di * 1024>{}, op.vh[di], wosrc(3, 4 + hb), l3b);
          else if constexpr (c == 1) {
            constexpr int seq = ks == 2 ? r : ks == 3 ? 2 + r : ks;
            go(IC<(seq < 4 ? L_KR : L_VR + 32768) + (seq & 3) * 1024>{}, op.vo[seq & 3], wosrc(4 + (seq >> 2), wid), lw);
          }
          else go(IC<L_VR + di * 1024>{}, op.vo[di], wosrc(6, wid), lw);
          if constexpr (r == 0 && c == 0 && ks < 5) rd0(IC<(ks < 5 ? ks + 3 : 0)>{});
          if constexpr (r == nrt - 1 && c == 0 && ks >= 4) { wf[1][2 * (ks - 4)] = rdw(IC<1>{}, IC<2 * ((ks + 4) & 3)>{}); wf[1][2 * (ks - 4) + 1] = rdw(IC<1>{}, IC<2 * ((ks + 4) & 3) + 1>{}); }
          if constexpr (r == 0 && c >= 1 && c < 6) wf[(c + 1) & 1][ks] = rdw(IC<(c < 6 ? c + 1 : 0)>{}, KS);
        });
        if constexpr (c == 0 && ks == 6) {
#pragma unroll
          for (int r = 0; r < nrt; ++r) { li[r] = __builtin_amdgcn_rcpf(li[r]); PIN(li[r]); }
        }
        if constexpr (c >= 1 && ks >= 3 && ks < 3 + nrt) fin(IC<(c >= 1 ? c - 1 : 0)>{}, ks - 3);
        if constexpr (c == 2 && ks == 4) store(0, res[0]);
        if constexpr (c >= 2 && ks == 6) store(c - 1, res[1]);
        SBAR();
      });
    });
    asm volatile("s_nop 7"); SBAR();
#pragma unroll
    for (int r = 0; r < nrt; ++r) fin(IC<6>{}, r);
    store(6, res[1]);
    waitv<0>();
    if constexpr (rt0 == 0) { if (wid == 3) poll(0, 0); }
    bar();
    const auto rs = __builtin_amdgcn_make_buffer_rsrc((void*)part, 0, 256 * 28 * 2304, 0x00020000);
    auto reduce = [&](int nt, int z) {
      const int rt = z < 128 ? z >> 5 : 4, lp = z < 128 ? z & 31 : ((z - 128) >> 2) * 8 + (z & 3);
      const unsigned off = (unsigned)((grp * 28 + nt) * 2304 + z * 16);
      u32x4 v[16];
#pragma unroll
      for (int hh = 0; hh < 16; ++hh) v[hh] = __builtin_amdgcn_raw_buffer_load_b128(rs, off, hh * (16 * 28 * 2304), 16);
      float sm[8];
#pragma unroll
      for (int j = 0; j < 8; ++j) sm[j] = 0.f;
#pragma unroll
      for (int hh = 0; hh < 16; ++hh)
#pragma unroll
        for (int j = 0; j < 4; ++j) {
          sm[2 * j] += __builtin_bit_cast(float, v[hh][j] << 16);
          sm[2 * j + 1] += __builtin_bit_cast(float, v[hh][j] & 0xffff0000u);
        }
      char* o = out + (size_t)(b * SQ + half * 72 + rt * 16 + ((2 * lp) & 15)) * 896 + (nt * 16 + (lp >> 3) * 4) * 2;
      *(u32x2*)o = (u32x2){cvtpk(sm[0], sm[1]), cvtpk(sm[2], sm[3])};
      *(u32x2*)(o + 896) = (u32x2){cvtpk(sm[4], sm[5]), cvtpk(sm[6], sm[7])};
    };
    AS3 volatile unsigned* rdy = (AS3 volatile unsigned*)(L0 + L_MISC + 968);
    const int t = w * 64 + (int)lane_id();
    if constexpr (rt0 == 0) {
      if (wid == 3) {
        echo(1, 1);
        poll(1, 1);
        *rdy = 1u;
        const u64 last = tv; tokLoad();
        if (__any(tv != last)) echo(0, 1);
      } else if (t < 144) reduce(4 * cbo(h >> 2) + (h & 3), t);
    } else if (w < 2) {
      const u64 t0 = __builtin_amdgcn_s_memrealtime();
      while (*rdy == 0u && __builtin_amdgcn_s_memrealtime() - t0 < 25000000ull) __builtin_amdgcn_s_sleep(1);
      __builtin_amdgcn_fence(__ATOMIC_ACQUIRE, "wavefront");
      if (t < 108) { const int ub = h * 108 + t, tl = ub / 144; reduce(4 * cbo(4 + (tl >> 2)) + (tl & 3), ub - tl * 144); }
    }
  };

  unsigned flagA = L0 + L_MISC + 960;
  PIN(flagA);

  if (role == 0) {
    bf16x8 qf[5][8], kf[6];
    unsigned ka0 = kr + fa0, ka1 = kr + (fa0 ^ 64);
    PIN(ka0); PIN(ka1);
    auto rdk = [&](int par, int k) { return lds128(((k & 1) ? ka1 : ka0) + (2 * par + (k >> 2)) * 16384 + ((k >> 1) & 1) * 2048); };
    kf[2] = rdk(0, 2); kf[3] = rdk(0, 3); kf[0] = rdk(0, 0); kf[1] = rdk(0, 1);
    unsigned qra = L0 + L_QX + lane * 16, qrb = L0 + L_Q4 + (g * 8 + (r16 & 7)) * 16;
    PIN(qra); PIN(qrb);
    auto rdqf = [&](auto SL) {
      constexpr int sl = SL.value, rt = slot_rt(sl), k = slot_k(sl);
      qf[rt][k] = rt < 4 ? lds128(qra + (rt * 8 + k) * 1024) : lds128(qrb + k * 512);
    };
    static_for<0, 8>(rdqf);
    f32x4 negm[5];
    f32x4 sc[4], s4[2];
#pragma unroll
    for (int rt = 0; rt < 5; ++rt) { negm[rt] = (f32x4){0.f, 0.f, 0.f, 0.f}; PIN(negm[rt]); }
    const unsigned gp = 2 * (w & 1) + (g >> 1);
    unsigned pwa = L0 + L_PA + (w >> 1) * 1024 + (gp * 16 + r16) * 16 + (g & 1) * 8;
    unsigned pw3 = pwa + (L_PB - L_PA);
    unsigned pwb = L0 + L_PB + 2048 + (w >> 1) * 512 + (gp * 8 + (r16 & 7)) * 16 + (g & 1) * 8;
    PIN(pwa); PIN(pw3); PIN(pwb);
    unsigned vo[4];
    const char* kgb[4] = {kg + kcb(0), kg + kcb(1), kg + kcb(2), kg + kcb(3)};
    vk[0] += 65536; vk[1] += 65536;
    SBAR(); asm volatile("s_nop 1"); SBAR();
    __builtin_amdgcn_s_setprio(1);
    auto interval = [&](int i, auto PAR, auto DOB, auto DOA, auto MODE) {
      constexpr int par = PAR.value, mode = MODE.value; constexpr bool dob = DOB.value, doa = DOA.value;
      constexpr unsigned pbi = par ^ 1;
      unsigned fl = 0;
      if constexpr (dob) fl = *(AS3 const unsigned*)(flagA + (par ^ 1) * 4);
      const char* ga = mode == 0 ? kg : wosrc(w >> 1, 4 * (w & 1) + 2 * (mode - 1));
      const char* gb = mode == 0 ? ga : ga + 32 * 896;
      float e0, e1, e2, e3, mx; unsigned c0, c1;
      auto S = [&](int rt, bool old) -> f32x4& { return rt < 4 ? sc[rt] : s4[old ? par ^ 1 : par]; };
      static_for<0, 40>([&](auto SL) {
        constexpr int sl = SL.value, G = sl >> 2, z = sl & 3, rt = slot_rt(sl), k = slot_k(sl);
        constexpr int kb = k < 6 ? k : k - 6;
        constexpr bool first = sl < 20 && z == 0;
        constexpr int dp = sl == 0 ? 0 : sl == 1 ? 1 : sl == 17 ? 2 : sl == 19 ? 3 : sl == 27 ? 4 : sl == 30 ? 5 : sl == 33 ? 6 : sl == 36 ? 7 : -1;
        if constexpr (doa && dob && sl == 0) waitv<8>();
        if constexpr (doa && dob && sl == 20 && mode != 2) waitv<8>();
        if constexpr (doa) {
          constexpr bool dk = dp >= 0;
          constexpr int d = dp < 0 ? 0 : dp;
          constexpr int imml = (par * 2 + (d >> 2)) * 16384 + (d & 3) * 1024, off = 0;
          if constexpr (dk) {
            const unsigned v = mode == 0 ? vk[d & 1] : vo[d & 3];
            const char* gs = mode == 0 ? kgb[d >> 1] : d < 4 ? ga : gb;
            if constexpr (first) mfma_init_dma<imml, off>(S(rt, false), kf[kb], qf[rt][k], negm[rt], v, gs, kr);
            else mfma_acc_dma<imml, off>(S(rt, false), kf[kb], qf[rt][k], v, gs, kr);
          } else {
            if constexpr (first) mfma_init(S(rt, false), kf[kb], qf[rt][k], negm[rt]); else mfma_acc(S(rt, false), kf[kb], qf[rt][k]);
          }
          if constexpr (!dob && sl < 32) rdqf(IC<(sl < 32 ? sl + 8 : 0)>{});
          if constexpr (sl == 0) kf[4] = rdk(par, 4);
          if constexpr (sl == 1) kf[5] = rdk(par, 5);
          if constexpr (sl == 16) kf[0] = rdk(par, 6);
          if constexpr (sl == 17) kf[1] = rdk(par, 7);
          if constexpr (mode == 0 && sl == 34) { vk[0] += 32768; PIN(vk[0]); }
          if constexpr (mode == 0 && sl == 37) { vk[1] += 32768; PIN(vk[1]); }
          if constexpr (mode != 2) {
            if constexpr (sl == 25) kf[2] = rdk(par ^ 1, 2);
            if constexpr (sl == 29) kf[3] = rdk(par ^ 1, 3);
            if constexpr (sl == 38) kf[0] = rdk(par ^ 1, 0);
            if constexpr (sl == 39) kf[1] = rdk(par ^ 1, 1);
          }
        }
        if constexpr (dob) {
          constexpr int je = G < 4 && z >= 2 ? G : (G == 5 && z < 2 ? 4 : -1);
          constexpr int jr = G >= 1 && G <= 4 && z == 0 ? G - 1 : (G == 5 && z == 2 ? 4 : -1);
          if constexpr (je >= 0) {
            if constexpr ((z & 1) == 0) { e0 = __builtin_amdgcn_exp2f(S(je, true)[0]); e1 = __builtin_amdgcn_exp2f(S(je, true)[1]); }
            else { e2 = __builtin_amdgcn_exp2f(S(je, true)[2]); e3 = __builtin_amdgcn_exp2f(S(je, true)[3]); c0 = cvtpk(e0, e1); }
          }
          if constexpr (jr >= 0) { c1 = cvtpk(e2, e3); ldsw64(jr < 3 ? pwa + pbi * 6144 + jr * 2048 : jr == 3 ? pw3 + pbi * 3072 : pwb + pbi * 3072, c0, c1); }
        }
        if constexpr (doa) {
          if constexpr (sl == 31) mx = fmax4(S(4, false));
          if constexpr (sl == 34) mx = fmax5(mx, sc[0]);
          if constexpr (sl == 39) mx = fmax5(mx, sc[1]);
        }
        SBAR();
        if constexpr (sl == 1 && dob) {
          if (__builtin_expect(rfl(fl) == (unsigned)i, 0)) {
            const unsigned ln = lane_id(), mrow = L0 + L_MISC + (ln & 15) * 4;
            const float aone = ln < 16 ? 1.f : 0.f;
            float mv[5];
#pragma unroll
            for (int r = 0; r < 5; ++r) mv[r] = *(AS3 const float*)(mrow + (par ^ 1) * 320 + r * 64);
            asm volatile("s_nop 7"); SBAR();
#pragma unroll
            for (int r = 0; r < 5; ++r) {
              const float old = -negm[r][0];
              const float mn = (mv[r] > old || i == 1) ? mv[r] + THR : old;
              const float dl = old - mn;
              bcast_add(S(r, true), aone, dl);
              if (r == 4 && doa) bcast_add(S(4, false), aone, dl);
              bcast_set(negm[r], aone, -mn);
            }
            hold5(negm[0], negm[1], negm[2], negm[3], negm[4]);
          }
          SBAR();
        }
      });
      if constexpr (doa) {
        mx = fmax5(mx, sc[2]);
        asm volatile("s_nop 2");
        mx = fmax5(mx, sc[3]);
        if (__builtin_expect(__any(mx > 0.f) || i == 0, 0)) {
          const unsigned mrow = L0 + L_MISC + (lane_id() & 15) * 4;
#pragma unroll
          for (int r = 0; r < 5; ++r) {
            const f32x4 sv = S(r, false);
            __builtin_amdgcn_ds_fmaxf((AS3 float*)(mrow + par * 320 + r * 64), fmax2(fmax3(sv[0], sv[1], sv[2]), sv[3]) - negm[r][0], 0, 0, false);
          }
          *(AS3 unsigned*)(flagA + par * 4) = (unsigned)(i + 1);
        }
      }
    };
    interval(0, IC<0>{}, IC<0>{}, IC<1>{}, IC<0>{}); bar();
    for (int i = 1; i <= 12; i += 2) {
      interval(i, IC<1>{}, IC<1>{}, IC<1>{}, IC<0>{}); bar();
      interval(i + 1, IC<0>{}, IC<1>{}, IC<1>{}, IC<0>{}); bar();
    }
    interval(13, IC<1>{}, IC<1>{}, IC<1>{}, IC<0>{});
    {
      const unsigned ln = lane_id();
#pragma unroll
      for (int pc = 0; pc < 4; ++pc) { vo[pc] = wovoff(ln, pc); PIN(vo[pc]); }
    }
    bar();
    interval(14, IC<0>{}, IC<1>{}, IC<1>{}, IC<1>{}); bar();
    interval(15, IC<1>{}, IC<1>{}, IC<1>{}, IC<2>{}); bar();
    interval(16, IC<0>{}, IC<1>{}, IC<0>{}, IC<2>{});
    __builtin_amdgcn_s_setprio(0);
    bar();
#pragma unroll
    for (int j = 0; j < 8; ++j) dma16(vo[j & 3], wosrc(2, 2 * w + (j >> 2)), L0 + L_VR + (2 * w) * 4096 + j * 1024);
    const OP op = opSetup();
    tokLoad(); mytok = *(AS3 const u64*)(L0 + L_MISC + 976);
    PIN(tv); PIN(mytok);
    waitv<0>();
    bar();
    outproj(op, IC<3>{}, IC<0>{});
  } else {
    f32x4 o[4][5];
#pragma unroll
    for (int dt = 0; dt < 4; ++dt)
#pragma unroll
      for (int rt = 0; rt < 5; ++rt) { o[dt][rt] = (f32x4){0.f, 0.f, 0.f, 0.f}; PIN(o[dt][rt]); }
    f32x4 lw = {0.f, 0.f, 0.f, 0.f}, l4 = {0.f, 0.f, 0.f, 0.f};
    bf16x8 ones = {0x3F80, 0x3F80, 0x3F80, 0x3F80, 0x3F80, 0x3F80, 0x3F80, 0x3F80}, lf;
    PIN(lw); PIN(l4); PIN(ones);
    const unsigned mrow = L0 + L_MISC + r16 * 4;
    float mthr[5], alpha[5]; bool resc = false;
#pragma unroll
    for (int rt = 0; rt < 5; ++rt) { mthr[rt] = 0.f; alpha[rt] = 1.f; }
    bf16x8 vf[4], pf[2][5];
    const unsigned va0 = (8 * g + q) * 128 + ((((q >> 1) & 1) | ((g & 1) << 1)) << 5) + p * 8;
    unsigned vad[4], vv[8];
#pragma unroll
    for (int dt = 0; dt < 4; ++dt) { vad[dt] = vr + (va0 ^ (dt << 5)); PIN(vad[dt]); }
#pragma unroll
    for (int n = 0; n < 8; ++n) { vv[n] = vvoff(n); PIN(vv[n]); }
    unsigned pra = L0 + L_PA + lane * 16, pr3 = L0 + L_PB + lane * 16, prb = L0 + L_PB + 2048 + (g * 8 + (r16 & 7)) * 16;
    unsigned prw0 = w < 3 ? pra + w * 2048 : pr3, prw1 = prw0 + (w < 3 ? 6144 : 3072), pr4 = prb + (r16 >> 3) * 512;
    unsigned owa = vr + 32768 + ((g >> 1) * 16 + r16) * 16 + (g & 1) * 8, owb = L0 + L_O4 + w * 1024 + ((g >> 1) * 8 + (r16 & 7)) * 16 + (g & 1) * 8;
    PIN(pra); PIN(pr3); PIN(prb); PIN(prw0); PIN(prw1); PIN(pr4); PIN(owa); PIN(owb);
    const OP op = opSetup();
    auto rdv = [&](int par, int n) { const unsigned a = vad[n & 3] + (2 * par + (n >> 2)) * 16384; return cat(ldstr(a), ldstr(a + 512)); };
    auto rdp = [&](unsigned pb, int hh, int rt) { return rt < 3 ? lds128(pra + pb * 6144 + (rt * 2 + hh) * 1024) : rt == 3 ? lds128(pr3 + pb * 3072 + hh * 1024) : lds128(prb + pb * 3072 + hh * 512); };
    auto interval = [&](int i, auto PAR, auto CARRY, auto DOC, auto DOM, auto MODE) {
      constexpr int par = PAR.value, mode = MODE.value; constexpr bool carry = CARRY.value, doc = DOC.value, dom = DOM.value;
      constexpr unsigned pb = par;
      unsigned fl = 0;
      const char* vga = vg + (size_t)TT((i - 1) & 15) * 32768;
      const char* vgb = vg + (size_t)TT(i & 15) * 32768;
      if constexpr (doc) {
        static_for<0, (mode == 2 ? 10 : 8)>([&](auto NN) {
          constexpr int nn = NN.value, n = (nn + 6) & 7;
          constexpr bool live = nn >= 2 || carry;
          if constexpr (nn == 0) { if constexpr (mode == 2) waitv<4>(); else waitv<8>(); }
          if constexpr (nn == 4) { if constexpr (mode == 2) waitv<0>(); else waitv<8>(); }
          static_for<0, 5>([&](auto RT) {
            constexpr int rt = RT.value;
            constexpr int dp = nn < 4 ? (rt == 3 ? nn : -1) : (nn == 6 || nn == 7) ? (rt == 1 ? 4 + 2 * (nn - 6) : rt == 3 ? 5 + 2 * (nn - 6) : -1) : -1;
            constexpr bool dv = dp >= 0 && (mode == 0 || (mode == 1 && dp < 4)) && (carry || dp >= 4);
            constexpr int d = dp < 0 ? 0 : dp, pc = d & 3;
            constexpr int imml = ((2 * par + 3 + (d >> 2)) & 3) * 16384 + pc * 1024;
            if constexpr (live) {
              if constexpr (dv) mfma_acc_dma<imml, 0>(o[n & 3][rt], vf[n & 3], pf[n >> 2][rt], vv[d ^ 4], d < 4 ? vga : vgb, vr);
              else mfma_acc(o[n & 3][rt], vf[n & 3], pf[n >> 2][rt]);
            } else if constexpr (dv) dma16(vv[d ^ 4], d < 4 ? vga : vgb, vr + imml);
            if constexpr (nn == 0) pf[0][rt] = rdp(pb, 0, rt);
            if constexpr (nn == 1 && rt == 4 && dom) fl = *(AS3 const unsigned*)(flagA + (par ^ 1) * 4);
            if constexpr (rt == 0 && nn < 8) vf[nn & 3] = rdv(par, nn);
            if constexpr (nn == 2 && rt >= 1 && rt <= 2) pf[1][rt - 1] = rdp(pb, 1, rt - 1);
            if constexpr (nn == 3 && rt >= 1 && rt <= 2) pf[1][rt + 1] = rdp(pb, 1, rt + 1);
            if constexpr (nn == 4 && rt == 1) pf[1][4] = rdp(pb, 1, 4);
            if constexpr (nn == 2 && rt == 4) lf = lds128(par ? prw1 : prw0);
            if constexpr (nn == 4 && rt == 2) { SBAR(); mfma_acc(lw, ones, lf); lf = lds128((par ? prw1 : prw0) + 1024); }
            if constexpr (nn == 5 && rt == 4) { SBAR(); mfma_acc(lw, ones, lf); lf = lds128(pr4 + pb * 3072); }
            if constexpr (nn == 7 && rt == 2) { SBAR(); mfma_acc(l4, ones, lf); }
            SBAR();
          });
          if constexpr (nn == 1) {
            if (__builtin_expect(resc, 0)) {
              asm volatile("s_nop 7"); SBAR();
#pragma unroll
              for (int dt = 0; dt < 4; ++dt)
#pragma unroll
                for (int rt = 0; rt < 5; ++rt) o[dt][rt] *= alpha[rt];
              lw *= (w == 0 ? alpha[0] : w == 1 ? alpha[1] : w == 2 ? alpha[2] : alpha[3]); l4 *= alpha[4];
              SBAR();
              asm volatile("s_nop 1");
            }
            SBAR();
          }
        });
      } else if constexpr (dom) fl = *(AS3 const unsigned*)(flagA + (par ^ 1) * 4);
      if constexpr (dom) {
        resc = rfl(fl) == (unsigned)i;
        if (__builtin_expect(resc, 0)) {
#pragma unroll
          for (int rt = 0; rt < 5; ++rt) {
            const float mv = *(AS3 const float*)(mrow + (par ^ 1) * 320 + rt * 64);
            const float mn = (mv > mthr[rt] || i == 1) ? mv + THR : mthr[rt];
            alpha[rt] = __builtin_amdgcn_exp2f(mthr[rt] - mn); mthr[rt] = mn;
          }
          if (i == 1) resc = false;
        }
      }
    };
    if (wid == 7) tokInit();
    waitv<2>();
    bar();
#pragma unroll
    for (int n = 0; n < 8; ++n) dma16(vv[n], vg + (size_t)TT(1) * 32768, vr + (2 + (n >> 2)) * 16384 + (n & 3) * 1024);
    interval(1, IC<1>{}, IC<0>{}, IC<0>{}, IC<1>{}, IC<0>{}); bar();
    interval(2, IC<0>{}, IC<0>{}, IC<1>{}, IC<1>{}, IC<0>{}); bar();
    for (int i = 3; i <= 14; i += 2) {
      interval(i, IC<1>{}, IC<1>{}, IC<1>{}, IC<1>{}, IC<0>{}); bar();
      interval(i + 1, IC<0>{}, IC<1>{}, IC<1>{}, IC<1>{}, IC<0>{}); bar();
    }
    interval(15, IC<1>{}, IC<1>{}, IC<1>{}, IC<1>{}, IC<0>{}); bar();
    interval(16, IC<0>{}, IC<1>{}, IC<1>{}, IC<1>{}, IC<1>{}); bar();
    interval(17, IC<1>{}, IC<1>{}, IC<1>{}, IC<0>{}, IC<2>{});
    {
      SBAR(); asm volatile("s_nop 7"); SBAR();
      *(AS3 float*)(mrow + 640 + w * 64) = lw[0];
      if (w == 0 && g == 0) __builtin_amdgcn_ds_faddf((AS3 float*)(L0 + L_MISC + 640 + 256 + (r16 & 7) * 4), l4[0], 0, 0, false);
#pragma unroll
      for (int rt = 0; rt < 5; ++rt)
#pragma unroll
        for (int dt = 0; dt < 4; ++dt) {
          const unsigned a = rt < 4 ? owa + (rt >> 1) * 16384 + ((rt & 1) * 2 + (dt >> 1)) * 1024 + (dt & 1) * 512 : owb + (dt >> 1) * 512 + (dt & 1) * 256;
          ldsw64(a, cvtpk(o[dt][rt][0], o[dt][rt][1]), cvtpk(o[dt][rt][2], o[dt][rt][3]));
        }
    }
    bar();
    outproj(op, IC<2>{}, IC<3>{});
  }
}

extern "C" void kernel_launch(void* const* d_in, const int* in_sizes, int n_in, void* d_out, int out_size, void* d_ws, size_t ws_size,
                              hipStream_t stream) {
  (void)in_sizes; (void)n_in; (void)out_size; (void)ws_size;
  (void)hipFuncSetAttribute((const void*)fused_attn, hipFuncAttributeMaxDynamicSharedMemorySize, (int)LDS_BYTES);
  char* part = (char*)d_ws;
  u64* sync = (u64*)((char*)d_ws + (32 << 20));
  hipLaunchKernelGGL(fused_attn, dim3(256), dim3(512), LDS_BYTES, stream, (const char*)d_in[0], (const char*)d_in[1], (const char*)d_in[2],
                     (const char*)d_in[3], (const char*)d_in[4], part, (char*)d_out, sync);
}
```
